```python
import torch

def _softmax(x, axis=-1):
    return torch.softmax(x, dim=axis)
def _gelu(x):
    return torch.nn.functional.gelu(x, approximate="tanh")
def _silu(x):
    return torch.nn.functional.silu(x)
def _sigmoid(x):
    return torch.sigmoid(x)
def _layernorm(x, axis=-1, eps=1e-5):
    m = x.mean(dim=axis, keepdim=True)
    v = x.var(dim=axis, keepdim=True, unbiased=False)
    return (x - m) / torch.sqrt(v + eps)
def _scatter_add(tgt, idx, src, axis=0):
    return tgt.clone().index_add_(axis, idx, src)
def _segment_sum(src, seg, n):
    out = torch.zeros((n,) + src.shape[1:], dtype=src.dtype)
    return out.index_add_(0, seg, src)
def _pad(x, pw):
    flat = tuple(v for pair in reversed(pw) for v in pair)
    return torch.nn.functional.pad(x, flat)

kernel_name = 'parallel_sum__ssm_conv_h3__glu_gated_glu__prompt'

def setup_inputs():

    _w = (lambda a: (lambda t: t.to(torch.bfloat16) if t.is_floating_point() else t)(torch.as_tensor(a)))
    def _A_setup():
        B, S, D = 1, 2048, 640
        ks = 4
        return {
            "q": _w((lambda *s: torch.randn(*s, dtype=torch.bfloat16) * 0.05)(B, S, D)),
            "k": _w((lambda *s: torch.randn(*s, dtype=torch.bfloat16) * 0.4)(B, S, D)),
            "v": _w((lambda *s: torch.randn(*s, dtype=torch.bfloat16) * 0.4)(B, S, D)),
            "shift_k": _w((lambda *s: torch.randn(*s, dtype=torch.bfloat16) * 0.4)(D, ks) * 0.5),
            "ssm_k": _w((lambda *s: torch.randn(*s, dtype=torch.bfloat16) * 1.6)(D, S) * (1.0 / S ** 0.5)),
            "ssm_D": _w((lambda *s: torch.randn(*s, dtype=torch.bfloat16) * 0.4)(D)),
        }
    _A_in = _A_setup()

    def _B_setup():
        B, S, D, F = 1, 2048, 640, 1728
        return {
            "x": _w((lambda *s: torch.randn(*s, dtype=torch.bfloat16) * 0.05)(B, S, D)),
            "w_gate": _w((lambda *s: torch.randn(*s, dtype=torch.bfloat16) * 0.05)(D, F)),
            "w_up": _w((lambda *s: torch.randn(*s, dtype=torch.bfloat16) * 0.05)(D, F)),
            "w_down": _w((lambda *s: torch.randn(*s, dtype=torch.bfloat16) * 0.05)(F, D)),
        }
    _B_in = _B_setup()

    return {
        "A_q": _A_in["q"],
        "A_k": _A_in["k"],
        "A_v": _A_in["v"],
        "A_shift_k": _A_in["shift_k"],
        "A_ssm_k": _A_in["ssm_k"],
        "A_ssm_D": _A_in["ssm_D"],
        "B_x": _B_in["x"],
        "B_w_gate": _B_in["w_gate"],
        "B_w_up": _B_in["w_up"],
        "B_w_down": _B_in["w_down"],
    }

def reference(A_q, A_k, A_v, A_shift_k, A_ssm_k, A_ssm_D, B_x, B_w_gate, B_w_up, B_w_down):
    def _A_ref(q, k, v, shift_k, ssm_k, ssm_D):
        B, S, D = q.shape
        ks = shift_k.shape[-1]
        k_pad = _pad(k, ((0, 0), (ks - 1, 0), (0, 0)))
        k_shift = torch.zeros_like(k)
        for t in range(ks):
            k_shift = k_shift + k_pad[:, t:t + S, :] * shift_k[:, ks - 1 - t][None, None, :]
        kv = k_shift * v
        L = 2 * S
        kv_pad = _pad(kv, ((0, 0), (0, S), (0, 0)))
        kf_pad = _pad(ssm_k, ((0, 0), (0, S)))
        kv_f = (lambda x, axes=(-2,-1): torch.fft.rfft2(x.float(), dim=axes))(kv_pad[:, None, :, :], axes=(1, 2))
        kf_f = (lambda x, axes=(-2,-1): torch.fft.rfft2(x.float(), dim=axes))(kf_pad[:, None, :], axes=(1, 2))
        kf_f = (lambda x, *d: x.permute(*d))(kf_f, 1, 2, 0)
        y_f = kv_f * kf_f[None, :, :, :]
        y = (lambda x, s, axes=(-2,-1): torch.fft.irfft2(x, s=s, dim=axes).to(torch.bfloat16))(y_f, (1, L), axes=(1, 2))[:, 0, :S, :]
        y = y + kv * ssm_D[None, None, :]
        return q * y

    def _B_ref(x, w_gate, w_up, w_down):
        g = torch.einsum("bsd,df->bsf", x, w_gate)
        u = torch.einsum("bsd,df->bsf", x, w_up)
        h = _sigmoid(g) * u
        return torch.einsum("bsf,fd->bsd", h, w_down)

    x = A_q
    xn = _layernorm(x, axis=-1)
    return x + _A_ref(xn, A_k, A_v, A_shift_k, A_ssm_k, A_ssm_D) + _B_ref(xn, B_w_gate, B_w_up, B_w_down)
```

```cpp
#include <hip/hip_runtime.h>
#include <hip/hip_bf16.h>
#include <stdint.h>

typedef unsigned short u16;
using bf16x8 = __attribute__((ext_vector_type(8))) short;
using s16x4  = __attribute__((ext_vector_type(4))) short;
using f32x4  = __attribute__((ext_vector_type(4))) float;
using f32x16 = __attribute__((ext_vector_type(16))) float;

constexpr int S = 2048, D = 640, F = 1728;
constexpr int NG1 = 16 * 18, NCV = 160, NTR = 27;

__device__ __forceinline__ float bf2f(u16 v) { return __uint_as_float((unsigned)v << 16); }
__device__ __forceinline__ u16 f2bf(float f) {
  __hip_bfloat16 b = __float2bfloat16(f);
  return *reinterpret_cast<u16*>(&b);
}
__device__ __forceinline__ float rbf(float f) { return bf2f(f2bf(f)); }

struct Params {
  const u16 *x, *k, *v, *sk, *hk, *Dv, *wg, *wu, *wd;
  u16 *out, *Hf, *Wf, *y2;
  float2* stats;
};

constexpr int A_BUF = 128 * 64 * 2;
constexpr int B_RS = 416;
constexpr int B_BUF = 64 * B_RS;
constexpr int LDS1 = 2 * A_BUF + 2 * B_BUF + 1024;
constexpr int ZOFF = 2080;
constexpr int ZLEN = 3200;
constexpr int ZB = (ZLEN / 32) * 40 * 2;
constexpr int CW = 528, CS = 1344, CCH = 8 * CS;

__device__ __forceinline__ int zaddr(int i) { return ((i >> 5) * 40 + (i & 31)) * 2; }

__device__ void gemm1(const Params& p, int bid, char* sm) {
  const int t = threadIdx.x, lane = t & 63, w = t >> 6;
  const int mt = bid / 18, ft = bid % 18;
  const int m0 = mt * 128, f0 = ft * 96;
  char* sA = sm; char* sB = sm + 2 * A_BUF;
  float2* sst = (float2*)(sm + 2 * A_BUF + 2 * B_BUF);
  for (int it = 0; it < 8; ++it) {
    int r = w * 32 + it * 4 + (lane >> 4);
    const u16* xr = p.x + (size_t)(m0 + r) * D;
    float vals[40]; float s = 0.f;
#pragma unroll
    for (int c = 0; c < 5; ++c) {
      bf16x8 v = *(const bf16x8*)(xr + ((lane & 15) + 16 * c) * 8);
#pragma unroll
      for (int j = 0; j < 8; ++j) { vals[c * 8 + j] = bf2f((u16)v[j]); s += vals[c * 8 + j]; }
    }
    for (int o = 1; o < 16; o <<= 1) s += __shfl_xor(s, o);
    float mean = s * (1.f / D), q = 0.f;
#pragma unroll
    for (int j = 0; j < 40; ++j) { float d = vals[j] - mean; q += d * d; }
    for (int o = 1; o < 16; o <<= 1) q += __shfl_xor(q, o);
    float var = rbf(q * (1.f / D));
    float den = rbf(sqrtf(rbf(var + 1e-5f)));
    if ((lane & 15) == 0) {
      float2 st = make_float2(rbf(mean), den);
      sst[r] = st;
      if (ft == 0) p.stats[m0 + r] = st;
    }
  }
  __syncthreads();
  float2 rst[4];
#pragma unroll
  for (int i = 0; i < 4; ++i) rst[i] = sst[(t >> 3) + 32 * i];

  bf16x8 ra[4], rb[6];
  auto gload = [&](int kt) {
#pragma unroll
    for (int i = 0; i < 4; ++i) {
      int r = (t >> 3) + 32 * i;
      ra[i] = *(const bf16x8*)(p.x + (size_t)(m0 + r) * D + kt * 64 + (t & 7) * 8);
    }
#pragma unroll
    for (int i = 0; i < 6; ++i) {
      int id = t + 256 * i, kr = id / 24, ch = id % 24;
      const u16* src = (ch < 12 ? p.wg : p.wu) + (size_t)(kt * 64 + kr) * F + f0 + (ch % 12) * 8;
      rb[i] = *(const bf16x8*)src;
    }
  };
  auto lstore = [&](int buf) {
#pragma unroll
    for (int i = 0; i < 4; ++i) {
      int r = (t >> 3) + 32 * i;
      bf16x8 o;
#pragma unroll
      for (int j = 0; j < 8; ++j) o[j] = (short)f2bf(rbf(bf2f((u16)ra[i][j]) - rst[i].x) / rst[i].y);
      *(bf16x8*)(sA + buf * A_BUF + r * 128 + (((t & 7) ^ (r & 7)) << 4)) = o;
    }
#pragma unroll
    for (int i = 0; i < 6; ++i) {
      int id = t + 256 * i, kr = id / 24, ch = id % 24;
      *(bf16x8*)(sB + buf * B_BUF + kr * B_RS + ((ch ^ (((kr >> 3) & 1) << 1)) << 4)) = rb[i];
    }
  };
  const int wm = w >> 1, wn = w & 1;
  f32x4 acc[4][6];
#pragma unroll
  for (int i = 0; i < 4; ++i)
#pragma unroll
    for (int j = 0; j < 6; ++j) acc[i][j] = f32x4{0, 0, 0, 0};
  gload(0); lstore(0); __syncthreads();
  const int g = lane >> 4, q = (lane & 15) >> 2, pp = lane & 3;
  for (int kt = 0; kt < 10; ++kt) {
    int buf = kt & 1;
    if (kt + 1 < 10) gload(kt + 1);
#pragma unroll
    for (int ks = 0; ks < 2; ++ks) {
      bf16x8 af[4], bfr[6];
#pragma unroll
      for (int i = 0; i < 4; ++i) {
        int r = wm * 64 + i * 16 + (lane & 15);
        af[i] = *(const bf16x8*)(sA + buf * A_BUF + r * 128 + (((ks * 4 + g) ^ (r & 7)) << 4));
      }
#pragma unroll
      for (int j = 0; j < 6; ++j) {
        int c16 = (j < 3 ? wn * 3 + j : 6 + wn * 3 + (j - 3));
        s16x4 lo, hi;
        {
          int row = 32 * ks + 8 * g + q;
          int ch = 2 * c16 + (pp >> 1);
          auto ptr = (__attribute__((address_space(3))) s16x4*)(sB + buf * B_BUF + row * B_RS + ((ch ^ (((row >> 3) & 1) << 1)) << 4) + 8 * (pp & 1));
          lo = __builtin_amdgcn_ds_read_tr16_b64_v4i16(ptr);
          row += 4;
          ptr = (__attribute__((address_space(3))) s16x4*)(sB + buf * B_BUF + row * B_RS + ((ch ^ (((row >> 3) & 1) << 1)) << 4) + 8 * (pp & 1));
          hi = __builtin_amdgcn_ds_read_tr16_b64_v4i16(ptr);
        }
        bfr[j] = bf16x8{lo[0], lo[1], lo[2], lo[3], hi[0], hi[1], hi[2], hi[3]};
      }
#pragma unroll
      for (int i = 0; i < 4; ++i)
#pragma unroll
        for (int j = 0; j < 6; ++j)
          acc[i][j] = __builtin_amdgcn_mfma_f32_16x16x32_bf16(af[i], bfr[j], acc[i][j], 0, 0, 0);
    }
    if (kt + 1 < 10) lstore(buf ^ 1);
    __syncthreads();
  }
  char* sH = sm;
#pragma unroll
  for (int i = 0; i < 4; ++i)
#pragma unroll
    for (int j = 0; j < 3; ++j)
#pragma unroll
      for (int e = 0; e < 4; ++e) {
        float gg = rbf(acc[i][j][e]), uu = rbf(acc[i][j + 3][e]);
        float sg = rbf(1.f / (1.f + __expf(-gg)));
        int r = wm * 64 + i * 16 + g * 4 + e, c = wn * 48 + j * 16 + (lane & 15);
        *(u16*)(sH + r * 208 + c * 2) = f2bf(sg * uu);
      }
  __syncthreads();
#pragma unroll
  for (int i = 0; i < 6; ++i) {
    int id = t + 256 * i, blk = id >> 6, l = id & 63;
    int m16 = blk / 3, k32 = blk % 3;
    int r = m16 * 16 + (l & 15), c = k32 * 32 + (l >> 4) * 8;
    bf16x8 v = *(const bf16x8*)(sH + r * 208 + c * 2);
    *(bf16x8*)(p.Hf + ((size_t)((mt * 8 + m16) * 54 + ft * 3 + k32) * 64 + l) * 8) = v;
  }
}

__device__ void convk(const Params& p, int bid, char* sm) {
  const int t = threadIdx.x, lane = t & 63, w = t >> 6;
  const int d0 = bid * 4;
  char* sZ = sm; char* sC = sm + 4 * ZB;
  for (int i = t; i < 4 * ZB / 16; i += 256) *(uint4*)(sZ + i * 16) = make_uint4(0, 0, 0, 0);
  __syncthreads();
  float wsh[4][4];
#pragma unroll
  for (int c = 0; c < 4; ++c)
#pragma unroll
    for (int j = 0; j < 4; ++j) wsh[c][j] = bf2f(p.sk[(d0 + c) * 4 + j]);
  for (int s = t; s < S; s += 256) {
    float ac[4] = {0, 0, 0, 0};
#pragma unroll
    for (int tt = 0; tt < 4; ++tt) {
      int ss = s - 3 + tt;
      if (ss >= 0) {
        s16x4 kk = *(const s16x4*)(p.k + (size_t)ss * D + d0);
#pragma unroll
        for (int c = 0; c < 4; ++c) ac[c] = rbf(ac[c] + rbf(bf2f((u16)kk[c]) * wsh[c][3 - tt]));
      }
    }
    s16x4 vv = *(const s16x4*)(p.v + (size_t)s * D + d0);
#pragma unroll
    for (int c = 0; c < 4; ++c) *(u16*)(sZ + c * ZB + zaddr(ZOFF - s)) = f2bf(ac[c] * bf2f((u16)vv[c]));
  }
  __syncthreads();
  const int d = d0 + w;
  const u16* hrow = p.hk + (size_t)d * S;
  char* cp = sC + w * CCH; const char* z = sZ + w * ZB;
  f32x16 a0, a1;
#pragma unroll
  for (int i = 0; i < 16; ++i) { a0[i] = 0; a1[i] = 0; }
  const int b = lane & 31, g = lane >> 5;
  for (int win = 0; win < 4; ++win) {
    int base = win * CW - 32;
    for (int gi = lane; gi < 70; gi += 64) {
      u16 e[16];
#pragma unroll
      for (int hh = 0; hh < 2; ++hh) {
        int idx = base + gi * 8 + hh * 8;
        bf16x8 v = bf16x8{0, 0, 0, 0, 0, 0, 0, 0};
        if (idx >= 0 && idx < S) v = *(const bf16x8*)(hrow + idx);
#pragma unroll
        for (int j = 0; j < 8; ++j) e[hh * 8 + j] = (u16)v[j];
      }
#pragma unroll
      for (int c = 0; c < 8; ++c) {
        bf16x8 o;
#pragma unroll
        for (int j = 0; j < 8; ++j) o[j] = (short)e[c + j];
        *(bf16x8*)(cp + c * CS + gi * 16) = o;
      }
    }
    __syncthreads();
    const char* ap = cp + (b & 7) * CS + ((b & ~7) + 8 * g) * 2;
    for (int st = 0; st < 33; ++st) {
      int k0 = base + st * 16;
      bf16x8 af = *(const bf16x8*)(ap + st * 32);
      bf16x8 b1 = *(const bf16x8*)(z + zaddr(ZOFF - 32 * (b + 32) + k0 + 8 * g));
      a1 = __builtin_amdgcn_mfma_f32_32x32x16_bf16(af, b1, a1, 0, 0, 0);
      if (win < 2) {
        bf16x8 b0 = *(const bf16x8*)(z + zaddr(ZOFF - 32 * b + k0 + 8 * g));
        a0 = __builtin_amdgcn_mfma_f32_32x32x16_bf16(af, b0, a0, 0, 0, 0);
      }
    }
    __syncthreads();
  }
  u16* so = (u16*)sC;
  float Dd = bf2f(p.Dv[d]);
#pragma unroll
  for (int tl = 0; tl < 2; ++tl)
#pragma unroll
    for (int r = 0; r < 16; ++r) {
      int s = 32 * (b + 32 * tl) + (r & 3) + 8 * (r >> 2) + 4 * g;
      float y = rbf(tl ? a1[r] : a0[r]);
      float kv = bf2f(*(const u16*)(z + zaddr(ZOFF - s)));
      so[s * 4 + w] = f2bf(y + rbf(kv * Dd));
    }
  __syncthreads();
  for (int s = t; s < S; s += 256) *(uint2*)(p.y2 + (size_t)s * D + d0) = *(const uint2*)(so + s * 4);
}

__device__ void trwd(const Params& p, int bid, char* sm) {
  const int t = threadIdx.x;
  u16* s = (u16*)sm;
  for (int id = t; id < 64 * 80; id += 256) {
    int r = id / 80, c = id % 80;
    *(bf16x8*)(s + r * 648 + c * 8) = *(const bf16x8*)(p.wd + (size_t)(bid * 64 + r) * D + c * 8);
  }
  __syncthreads();
  for (int id = t; id < 2 * 40 * 64; id += 256) {
    int l = id & 63, blk = id >> 6, kt = blk / 40, nt = blk % 40;
    bf16x8 o;
#pragma unroll
    for (int j = 0; j < 8; ++j) o[j] = (short)s[(kt * 32 + (l >> 4) * 8 + j) * 648 + nt * 16 + (l & 15)];
    *(bf16x8*)(p.Wf + ((size_t)(nt * 54 + bid * 2 + kt) * 64 + l) * 8) = o;
  }
}

__global__ __launch_bounds__(256, 2) void k1(Params p) {
  __shared__ __attribute__((aligned(16))) char sm[LDS1];
  int b = blockIdx.x;
  if (b < NCV) convk(p, b, sm);
  else if (b < NCV + NG1) gemm1(p, b - NCV, sm);
  else trwd(p, b - NCV - NG1, sm);
}

__global__ __launch_bounds__(256, 1) void k2(Params p) {
  __shared__ __attribute__((aligned(16))) float red[4][64][81];
  const int t = threadIdx.x, lane = t & 63, w = t >> 6;
  const int mt = blockIdx.x >> 3, nt = blockIdx.x & 7;
  f32x4 acc[4][5];
#pragma unroll
  for (int i = 0; i < 4; ++i)
#pragma unroll
    for (int j = 0; j < 5; ++j) acc[i][j] = f32x4{0, 0, 0, 0};
  for (int kt = w; kt < 54; kt += 4) {
    bf16x8 a[4], bb[5];
#pragma unroll
    for (int i = 0; i < 4; ++i) a[i] = *(const bf16x8*)(p.Hf + ((size_t)((mt * 4 + i) * 54 + kt) * 64 + lane) * 8);
#pragma unroll
    for (int j = 0; j < 5; ++j) bb[j] = *(const bf16x8*)(p.Wf + ((size_t)((nt * 5 + j) * 54 + kt) * 64 + lane) * 8);
#pragma unroll
    for (int i = 0; i < 4; ++i)
#pragma unroll
      for (int j = 0; j < 5; ++j) acc[i][j] = __builtin_amdgcn_mfma_f32_16x16x32_bf16(a[i], bb[j], acc[i][j], 0, 0, 0);
  }
#pragma unroll
  for (int i = 0; i < 4; ++i)
#pragma unroll
    for (int j = 0; j < 5; ++j)
#pragma unroll
      for (int e = 0; e < 4; ++e) red[w][i * 16 + (lane >> 4) * 4 + e][j * 16 + (lane & 15)] = acc[i][j][e];
  __syncthreads();
  for (int id = t; id < 64 * 80; id += 256) {
    int r = id / 80, c = id % 80;
    float glu = rbf(red[0][r][c] + red[1][r][c] + red[2][r][c] + red[3][r][c]);
    size_t o = (size_t)(mt * 64 + r) * D + nt * 80 + c;
    float2 st = p.stats[mt * 64 + r];
    float xv = bf2f(p.x[o]);
    float xn = rbf(rbf(xv - st.x) / st.y);
    float a = rbf(xn * bf2f(p.y2[o]));
    p.out[o] = f2bf(rbf(xv + a) + glu);
  }
}

extern "C" void kernel_launch(void* const* d_in, const int* in_sizes, int n_in,
                              void* d_out, int out_size, void* d_ws, size_t ws_size,
                              hipStream_t stream) {
  Params p;
  p.x = (const u16*)d_in[0]; p.k = (const u16*)d_in[1]; p.v = (const u16*)d_in[2];
  p.sk = (const u16*)d_in[3]; p.hk = (const u16*)d_in[4]; p.Dv = (const u16*)d_in[5];
  p.wg = (const u16*)d_in[7]; p.wu = (const u16*)d_in[8]; p.wd = (const u16*)d_in[9];
  p.out = (u16*)d_out;
  char* ws = (char*)d_ws;
  p.Hf = (u16*)ws;
  p.Wf = (u16*)(ws + 8u * 1024 * 1024);
  p.y2 = (u16*)(ws + 12u * 1024 * 1024);
  p.stats = (float2*)(ws + 16u * 1024 * 1024);
  k1<<<NCV + NG1 + NTR, 256, 0, stream>>>(p);
  k2<<<256, 256, 0, stream>>>(p);
}
```

```cpp
#include <hip/hip_runtime.h>
#include <hip/hip_bf16.h>
#include <stdint.h>

typedef unsigned short u16;
using bf16x8 = __attribute__((ext_vector_type(8))) short;
using s16x4  = __attribute__((ext_vector_type(4))) short;
using f32x4  = __attribute__((ext_vector_type(4))) float;
using f32x16 = __attribute__((ext_vector_type(16))) float;

constexpr int S = 2048, D = 640, F = 1728;
constexpr int NG1 = 16 * 18, NCV = 160, NTR = 54;

__device__ __forceinline__ float bf2f(u16 v) { return __uint_as_float((unsigned)v << 16); }
__device__ __forceinline__ u16 f2bf(float f) {
  __hip_bfloat16 b = __float2bfloat16(f);
  return *reinterpret_cast<u16*>(&b);
}
__device__ __forceinline__ float rbf(float f) { return bf2f(f2bf(f)); }

struct Params {
  const u16 *x, *k, *v, *sk, *hk, *Dv, *wg, *wu, *wd;
  u16 *out, *Hf, *Wf, *y2, *xn, *kvT;
  float2* stats;
};

constexpr int A_BUF = 128 * 64 * 2;
constexpr int B_RS = 384;
constexpr int B_BUF = 64 * B_RS;
constexpr int LDS1 = 2 * A_BUF + 2 * B_BUF;
#define BSW(r) (((((r) >> 1) & 1) | (((r) >> 2) & 2)) << 1)
constexpr int ZOFF = 2080;
constexpr int ZLEN = 3200;
constexpr int ZB = (ZLEN / 32) * 40 * 2;
constexpr int CW = 528, CS = 1344, CCH = 8 * CS;

__device__ __forceinline__ int zaddr(int i) { return ((i >> 5) * 40 + (i & 31)) * 2; }

__device__ void gemm1(const Params& p, int bid, char* sm) {
  const int t = threadIdx.x, lane = t & 63, w = t >> 6;
  const int mt = bid / 18, ft = bid % 18;
  const int m0 = mt * 128, f0 = ft * 96;
  char* sA = sm; char* sB = sm + 2 * A_BUF;
  bf16x8 ra[4], rb[6];
  auto gload = [&](int kt) {
#pragma unroll
    for (int i = 0; i < 4; ++i) {
      int r = (t >> 3) + 32 * i;
      ra[i] = *(const bf16x8*)(p.xn + (size_t)(m0 + r) * D + kt * 64 + (t & 7) * 8);
    }
#pragma unroll
    for (int i = 0; i < 6; ++i) {
      int id = t + 256 * i, kr = id / 24, ch = id % 24;
      const u16* src = (ch < 12 ? p.wg : p.wu) + (size_t)(kt * 64 + kr) * F + f0 + (ch % 12) * 8;
      rb[i] = *(const bf16x8*)src;
    }
  };
  auto lstore = [&](int buf) {
#pragma unroll
    for (int i = 0; i < 4; ++i) {
      int r = (t >> 3) + 32 * i;
      bf16x8 o = ra[i];
      *(bf16x8*)(sA + buf * A_BUF + r * 128 + (((t & 7) ^ (r & 7)) << 4)) = o;
    }
#pragma unroll
    for (int i = 0; i < 6; ++i) {
      int id = t + 256 * i, kr = id / 24, ch = id % 24;
      *(bf16x8*)(sB + buf * B_BUF + kr * B_RS + ((ch ^ BSW(kr)) << 4)) = rb[i];
    }
  };
  const int wm = w >> 1, wn = w & 1;
  f32x4 acc[4][6];
#pragma unroll
  for (int i = 0; i < 4; ++i)
#pragma unroll
    for (int j = 0; j < 6; ++j) acc[i][j] = f32x4{0, 0, 0, 0};
  gload(0); lstore(0); __syncthreads();
  const int g = lane >> 4, q = (lane & 15) >> 2, pp = lane & 3;
  for (int kt = 0; kt < 10; ++kt) {
    int buf = kt & 1;
    if (kt + 1 < 10) gload(kt + 1);
#pragma unroll
    for (int ks = 0; ks < 2; ++ks) {
      bf16x8 af[4], bfr[6];
#pragma unroll
      for (int i = 0; i < 4; ++i) {
        int r = wm * 64 + i * 16 + (lane & 15);
        af[i] = *(const bf16x8*)(sA + buf * A_BUF + r * 128 + (((ks * 4 + g) ^ (r & 7)) << 4));
      }
#pragma unroll
      for (int j = 0; j < 6; ++j) {
        int c16 = (j < 3 ? wn * 3 + j : 6 + wn * 3 + (j - 3));
        s16x4 lo, hi;
        {
          int row = 32 * ks + 8 * g + q;
          int ch = 2 * c16 + (pp >> 1);
          auto ptr = (__attribute__((address_space(3))) s16x4*)(sB + buf * B_BUF + row * B_RS + ((ch ^ BSW(row)) << 4) + 8 * (pp & 1));
          lo = __builtin_amdgcn_ds_read_tr16_b64_v4i16(ptr);
          row += 4;
          ptr = (__attribute__((address_space(3))) s16x4*)(sB + buf * B_BUF + row * B_RS + ((ch ^ BSW(row)) << 4) + 8 * (pp & 1));
          hi = __builtin_amdgcn_ds_read_tr16_b64_v4i16(ptr);
        }
        bfr[j] = bf16x8{lo[0], lo[1], lo[2], lo[3], hi[0], hi[1], hi[2], hi[3]};
      }
#pragma unroll
      for (int i = 0; i < 4; ++i)
#pragma unroll
        for (int j = 0; j < 6; ++j)
          acc[i][j] = __builtin_amdgcn_mfma_f32_16x16x32_bf16(af[i], bfr[j], acc[i][j], 0, 0, 0);
    }
    if (kt + 1 < 10) lstore(buf ^ 1);
    __syncthreads();
  }
  char* sH = sm;
#pragma unroll
  for (int i = 0; i < 4; ++i)
#pragma unroll
    for (int j = 0; j < 3; ++j)
#pragma unroll
      for (int e = 0; e < 4; ++e) {
        float gg = rbf(acc[i][j][e]), uu = rbf(acc[i][j + 3][e]);
        float sg = rbf(1.f / (1.f + __expf(-gg)));
        int r = wm * 64 + i * 16 + g * 4 + e, c = wn * 48 + j * 16 + (lane & 15);
        *(u16*)(sH + r * 208 + c * 2) = f2bf(sg * uu);
      }
  __syncthreads();
#pragma unroll
  for (int i = 0; i < 6; ++i) {
    int id = t + 256 * i, blk = id >> 6, l = id & 63;
    int m16 = blk / 3, k32 = blk % 3;
    int r = m16 * 16 + (l & 15), c = k32 * 32 + (l >> 4) * 8;
    bf16x8 v = *(const bf16x8*)(sH + r * 208 + c * 2);
    *(bf16x8*)(p.Hf + ((size_t)((mt * 8 + m16) * 54 + ft * 3 + k32) * 64 + l) * 8) = v;
  }
}

__device__ void convk(const Params& p, int bid, char* sm) {
  const int t = threadIdx.x, lane = t & 63, w = t >> 6;
  const int d0 = bid * 4;
  char* sZ = sm; char* sC = sm + 4 * ZB;
  for (int i = t; i < 4 * ZB / 16; i += 256) *(uint4*)(sZ + i * 16) = make_uint4(0, 0, 0, 0);
  __syncthreads();
  {
    const u16* src = p.kvT + (size_t)(d0 + w) * S;
    bf16x8 kv4[4];
#pragma unroll
    for (int i = 0; i < 4; ++i) kv4[i] = *(const bf16x8*)(src + (lane + 64 * i) * 8);
#pragma unroll
    for (int i = 0; i < 4; ++i)
#pragma unroll
      for (int j = 0; j < 8; ++j) *(u16*)(sZ + w * ZB + zaddr(ZOFF - ((lane + 64 * i) * 8 + j))) = (u16)kv4[i][j];
  }
  __syncthreads();
  const int d = d0 + w;
  const u16* hrow = p.hk + (size_t)d * S;
  char* cp = sC + w * CCH; const char* z = sZ + w * ZB;
  f32x16 a0, a1;
#pragma unroll
  for (int i = 0; i < 16; ++i) { a0[i] = 0; a1[i] = 0; }
  const int b = lane & 31, g = lane >> 5;
  for (int win = 0; win < 4; ++win) {
    int base = win * CW - 32;
    for (int gi = lane; gi < 70; gi += 64) {
      u16 e[16];
#pragma unroll
      for (int hh = 0; hh < 2; ++hh) {
        int idx = base + gi * 8 + hh * 8;
        bf16x8 v = bf16x8{0, 0, 0, 0, 0, 0, 0, 0};
        if (idx >= 0 && idx < S) v = *(const bf16x8*)(hrow + idx);
#pragma unroll
        for (int j = 0; j < 8; ++j) e[hh * 8 + j] = (u16)v[j];
      }
#pragma unroll
      for (int c = 0; c < 8; ++c) {
        bf16x8 o;
#pragma unroll
        for (int j = 0; j < 8; ++j) o[j] = (short)e[c + j];
        *(bf16x8*)(cp + c * CS + gi * 16) = o;
      }
    }
    __syncthreads();
    const char* ap = cp + (b & 7) * CS + ((b & ~7) + 8 * g) * 2;
#pragma unroll 11
    for (int st = 0; st < 33; ++st) {
      int k0 = base + st * 16;
      bf16x8 af = *(const bf16x8*)(ap + st * 32);
      bf16x8 b1 = *(const bf16x8*)(z + zaddr(ZOFF - 32 * (b + 32) + k0 + 8 * g));
      a1 = __builtin_amdgcn_mfma_f32_32x32x16_bf16(af, b1, a1, 0, 0, 0);
      if (win < 2) {
        bf16x8 b0 = *(const bf16x8*)(z + zaddr(ZOFF - 32 * b + k0 + 8 * g));
        a0 = __builtin_amdgcn_mfma_f32_32x32x16_bf16(af, b0, a0, 0, 0, 0);
      }
    }
    __syncthreads();
  }
  u16* so = (u16*)sC;
  float Dd = bf2f(p.Dv[d]);
#pragma unroll
  for (int tl = 0; tl < 2; ++tl)
#pragma unroll
    for (int r = 0; r < 16; ++r) {
      int s = 32 * (b + 32 * tl) + (r & 3) + 8 * (r >> 2) + 4 * g;
      float y = rbf(tl ? a1[r] : a0[r]);
      float kv = bf2f(*(const u16*)(z + zaddr(ZOFF - s)));
      so[s * 4 + w] = f2bf(y + rbf(kv * Dd));
    }
  __syncthreads();
  for (int s = t; s < S; s += 256) *(uint2*)(p.y2 + (size_t)s * D + d0) = *(const uint2*)(so + s * 4);
}

__device__ void trwd(const Params& p, int bid, char* sm) {
  const int t = threadIdx.x;
  u16* s = (u16*)sm;
  for (int id = t; id < 32 * 80; id += 256) {
    int r = id / 80, c = id % 80;
    *(bf16x8*)(s + r * 648 + c * 8) = *(const bf16x8*)(p.wd + (size_t)(bid * 32 + r) * D + c * 8);
  }
  __syncthreads();
  for (int id = t; id < 40 * 64; id += 256) {
    int l = id & 63, blk = id >> 6, kt = blk / 40, nt = blk % 40;
    bf16x8 o;
#pragma unroll
    for (int j = 0; j < 8; ++j) o[j] = (short)s[(kt * 32 + (l >> 4) * 8 + j) * 648 + nt * 16 + (l & 15)];
    *(bf16x8*)(p.Wf + ((size_t)(nt * 54 + bid + kt) * 64 + l) * 8) = o;
  }
}

__global__ __launch_bounds__(256) void k0(Params p) {
  __shared__ __attribute__((aligned(16))) char sm[32 * 648 * 2];
  int b = blockIdx.x;
  if (b >= 832) { trwd(p, b - 832, sm); return; }
  if (b >= 512) {
    b -= 512;
    const int s0 = (b / 10) * 64, d0 = (b % 10) * 64, t = threadIdx.x;
    u16* st = (u16*)sm;
    const int c8 = t & 7, rr = t >> 3, d = d0 + c8 * 8;
    bf16x8 kk[2][4], vv[2];
#pragma unroll
    for (int h = 0; h < 2; ++h) {
      int s = s0 + rr + 32 * h;
#pragma unroll
      for (int tt = 0; tt < 4; ++tt) {
        int ss = s - 3 + tt;
        kk[h][tt] = bf16x8{0, 0, 0, 0, 0, 0, 0, 0};
        if (ss >= 0) kk[h][tt] = *(const bf16x8*)(p.k + (size_t)ss * D + d);
      }
      vv[h] = *(const bf16x8*)(p.v + (size_t)s * D + d);
    }
#pragma unroll
    for (int j = 0; j < 8; ++j) {
      s16x4 wv = *(const s16x4*)(p.sk + (d + j) * 4);
#pragma unroll
      for (int h = 0; h < 2; ++h) {
        float ac = 0.f;
#pragma unroll
        for (int tt = 0; tt < 4; ++tt) ac = rbf(ac + rbf(bf2f((u16)kk[h][tt][j]) * bf2f((u16)wv[3 - tt])));
        st[(c8 * 8 + j) * 72 + rr + 32 * h] = f2bf(ac * bf2f((u16)vv[h][j]));
      }
    }
    __syncthreads();
#pragma unroll
    for (int i = 0; i < 2; ++i) {
      int id = t + 256 * i, dd = id >> 3, c = id & 7;
      *(bf16x8*)(p.kvT + (size_t)(d0 + dd) * S + s0 + c * 8) = *(const bf16x8*)(st + dd * 72 + c * 8);
    }
    return;
  }
  const int lane = threadIdx.x & 63, r = b * 4 + (threadIdx.x >> 6);
  unsigned raw[5]; float v[10], s = 0.f;
#pragma unroll
  for (int c = 0; c < 5; ++c) raw[c] = *(const unsigned*)(p.x + (size_t)r * D + c * 128 + lane * 2);
#pragma unroll
  for (int c = 0; c < 5; ++c) { v[2 * c] = bf2f((u16)raw[c]); v[2 * c + 1] = bf2f((u16)(raw[c] >> 16)); s += v[2 * c] + v[2 * c + 1]; }
  for (int o = 1; o < 64; o <<= 1) s += __shfl_xor(s, o);
  float mean = s * (1.f / D), q = 0.f;
#pragma unroll
  for (int j = 0; j < 10; ++j) { float d = v[j] - mean; q += d * d; }
  for (int o = 1; o < 64; o <<= 1) q += __shfl_xor(q, o);
  float den = rbf(sqrtf(rbf(rbf(q * (1.f / D)) + 1e-5f))), mb = rbf(mean);
  if (lane == 0) p.stats[r] = make_float2(mb, den);
#pragma unroll
  for (int c = 0; c < 5; ++c)
    *(unsigned*)(p.xn + (size_t)r * D + c * 128 + lane * 2) =
        (unsigned)f2bf(rbf(v[2 * c] - mb) / den) | ((unsigned)f2bf(rbf(v[2 * c + 1] - mb) / den) << 16);
}

__global__ __launch_bounds__(256, 2) void k1(Params p) {
  __shared__ __attribute__((aligned(16))) char sm[LDS1];
  int b = blockIdx.x;
  if (b < NCV) convk(p, b, sm);
  else if (b < NCV + NG1) gemm1(p, b - NCV, sm);
}

__global__ __launch_bounds__(256, 1) void k2(Params p) {
  __shared__ __attribute__((aligned(16))) float red[4][64][81];
  const int t = threadIdx.x, lane = t & 63, w = t >> 6;
  const int mt = blockIdx.x >> 3, nt = blockIdx.x & 7;
  f32x4 acc[4][5];
#pragma unroll
  for (int i = 0; i < 4; ++i)
#pragma unroll
    for (int j = 0; j < 5; ++j) acc[i][j] = f32x4{0, 0, 0, 0};
  bf16x8 a[3][4], bb[3][5];
  auto ld = [&](int sl, int kt) {
    if (kt > 53) kt = 53;
#pragma unroll
    for (int i = 0; i < 4; ++i) a[sl][i] = *(const bf16x8*)(p.Hf + ((size_t)((mt * 4 + i) * 54 + kt) * 64 + lane) * 8);
#pragma unroll
    for (int j = 0; j < 5; ++j) bb[sl][j] = *(const bf16x8*)(p.Wf + ((size_t)((nt * 5 + j) * 54 + kt) * 64 + lane) * 8);
  };
  ld(0, w); ld(1, w + 4);
  __builtin_amdgcn_sched_barrier(0);
#pragma unroll
  for (int it = 0; it < 14; ++it) {
    ld((it + 2) % 3, w + 4 * (it + 2));
    __builtin_amdgcn_sched_barrier(0);
    if (w + 4 * it < 54) {
#pragma unroll
      for (int i = 0; i < 4; ++i)
#pragma unroll
        for (int j = 0; j < 5; ++j) acc[i][j] = __builtin_amdgcn_mfma_f32_16x16x32_bf16(a[it % 3][i], bb[it % 3][j], acc[i][j], 0, 0, 0);
    }
  }
#pragma unroll
  for (int i = 0; i < 4; ++i)
#pragma unroll
    for (int j = 0; j < 5; ++j)
#pragma unroll
      for (int e = 0; e < 4; ++e) red[w][i * 16 + (lane >> 4) * 4 + e][j * 16 + (lane & 15)] = acc[i][j][e];
  __syncthreads();
  s16x4 xx[5], yy[5]; float2 sts[5];
#pragma unroll
  for (int i = 0; i < 5; ++i) {
    int id = t + 256 * i, r = id / 20, c = (id % 20) * 4;
    size_t o = (size_t)(mt * 64 + r) * D + nt * 80 + c;
    xx[i] = *(const s16x4*)(p.x + o); yy[i] = *(const s16x4*)(p.y2 + o); sts[i] = p.stats[mt * 64 + r];
  }
#pragma unroll
  for (int i = 0; i < 5; ++i) {
    int id = t + 256 * i, r = id / 20, c = (id % 20) * 4;
    size_t o = (size_t)(mt * 64 + r) * D + nt * 80 + c;
    s16x4 ov;
#pragma unroll
    for (int e = 0; e < 4; ++e) {
      float glu = rbf(red[0][r][c + e] + red[1][r][c + e] + red[2][r][c + e] + red[3][r][c + e]);
      float xv = bf2f((u16)xx[i][e]);
      float xn = rbf(rbf(xv - sts[i].x) / sts[i].y);
      float a = rbf(xn * bf2f((u16)yy[i][e]));
      ov[e] = (short)f2bf(rbf(xv + a) + glu);
    }
    *(s16x4*)(p.out + o) = ov;
  }
}

extern "C" void kernel_launch(void* const* d_in, const int* in_sizes, int n_in,
                              void* d_out, int out_size, void* d_ws, size_t ws_size,
                              hipStream_t stream) {
  Params p;
  p.x = (const u16*)d_in[0]; p.k = (const u16*)d_in[1]; p.v = (const u16*)d_in[2];
  p.sk = (const u16*)d_in[3]; p.hk = (const u16*)d_in[4]; p.Dv = (const u16*)d_in[5];
  p.wg = (const u16*)d_in[7]; p.wu = (const u16*)d_in[8]; p.wd = (const u16*)d_in[9];
  p.out = (u16*)d_out;
  char* ws = (char*)d_ws;
  p.Hf = (u16*)ws;
  p.Wf = (u16*)(ws + 8u * 1024 * 1024);
  p.y2 = (u16*)(ws + 12u * 1024 * 1024);
  p.stats = (float2*)(ws + 16u * 1024 * 1024);
  p.xn = (u16*)(ws + 20u * 1024 * 1024);
  p.kvT = (u16*)(ws + 24u * 1024 * 1024);
  k0<<<832 + NTR, 256, 0, stream>>>(p);
  k1<<<NCV + NG1, 256, 0, stream>>>(p);
  k2<<<256, 256, 0, stream>>>(p);
}
```

```cpp
#include <hip/hip_runtime.h>
#include <hip/hip_bf16.h>
#include <stdint.h>

typedef unsigned short u16;
using bf16x8 = __attribute__((ext_vector_type(8))) short;
using s16x4  = __attribute__((ext_vector_type(4))) short;
using f32x4  = __attribute__((ext_vector_type(4))) float;
using f32x16 = __attribute__((ext_vector_type(16))) float;

constexpr int S = 2048, D = 640, F = 1728;
constexpr int NG1 = 16 * 18, NCV = 160;
constexpr int N_LN = 128, N_KV = 320, N_WT = 174;

__device__ __forceinline__ float bf2f(u16 v) { return __uint_as_float((unsigned)v << 16); }
__device__ __forceinline__ u16 f2bf(float f) {
  __hip_bfloat16 b = __float2bfloat16(f);
  return *reinterpret_cast<u16*>(&b);
}
__device__ __forceinline__ float rbf(float f) { return bf2f(f2bf(f)); }

struct Params {
  const u16 *x, *k, *v, *sk, *hk, *Dv, *wg, *wu, *wd;
  u16 *out, *Hf, *Wdf, *y2, *Xf, *kvT, *Wgf, *Wuf;
  float2* stats;
};

template <int NC>
__device__ __forceinline__ void tile_w(const u16* src, int N, u16* dst, int KT, int kt, int n0, u16* s) {
  const int t = threadIdx.x;
  constexpr int RS = NC + 8, CPR = NC / 8, TOT = 32 * CPR;
  bf16x8 v[(TOT + 255) / 256];
#pragma unroll
  for (int i = 0; i < (TOT + 255) / 256; ++i) {
    int id = t + 256 * i;
    if (id < TOT) v[i] = *(const bf16x8*)(src + (size_t)(kt * 32 + id / CPR) * N + n0 + (id % CPR) * 8);
  }
#pragma unroll
  for (int i = 0; i < (TOT + 255) / 256; ++i) {
    int id = t + 256 * i;
    if (id < TOT) *(bf16x8*)(s + (id / CPR) * RS + (id % CPR) * 8) = v[i];
  }
  __syncthreads();
#pragma unroll
  for (int i = 0; i < (TOT + 255) / 256; ++i) {
    int id = t + 256 * i;
    if (id < TOT) {
      int l = id & 63, nt = id >> 6;
      bf16x8 o;
#pragma unroll
      for (int j = 0; j < 8; ++j) o[j] = (short)s[((l >> 4) * 8 + j) * RS + nt * 16 + (l & 15)];
      *(bf16x8*)(dst + ((size_t)((n0 / 16 + nt) * KT + kt) * 64 + l) * 8) = o;
    }
  }
}

__global__ __launch_bounds__(256) void k0(Params p) {
  __shared__ __attribute__((aligned(16))) u16 sm[32 * 648];
  int b = blockIdx.x;
  const int t = threadIdx.x, lane = t & 63, w = t >> 6;
  if (b < N_LN) {
    unsigned raw[4][5];
#pragma unroll
    for (int i = 0; i < 4; ++i)
#pragma unroll
      for (int c = 0; c < 5; ++c) raw[i][c] = *(const unsigned*)(p.x + (size_t)(b * 16 + w * 4 + i) * D + c * 128 + lane * 2);
#pragma unroll
    for (int i = 0; i < 4; ++i) {
      int rl = w * 4 + i;
      float v[10], s = 0.f;
#pragma unroll
      for (int c = 0; c < 5; ++c) { v[2 * c] = bf2f((u16)raw[i][c]); v[2 * c + 1] = bf2f((u16)(raw[i][c] >> 16)); s += v[2 * c] + v[2 * c + 1]; }
      for (int o = 1; o < 64; o <<= 1) s += __shfl_xor(s, o);
      float mean = s * (1.f / D), q = 0.f;
#pragma unroll
      for (int j = 0; j < 10; ++j) { float d = v[j] - mean; q += d * d; }
      for (int o = 1; o < 64; o <<= 1) q += __shfl_xor(q, o);
      float den = rbf(sqrtf(rbf(rbf(q * (1.f / D)) + 1e-5f))), mb = rbf(mean);
      if (lane == 0) p.stats[b * 16 + rl] = make_float2(mb, den);
#pragma unroll
      for (int c = 0; c < 5; ++c)
        *(unsigned*)(sm + rl * 648 + c * 128 + lane * 2) =
            (unsigned)f2bf(rbf(v[2 * c] - mb) / den) | ((unsigned)f2bf(rbf(v[2 * c + 1] - mb) / den) << 16);
    }
    __syncthreads();
#pragma unroll
    for (int i = 0; i < 5; ++i) {
      int id = t + 256 * i, l = id & 63, kt = id >> 6;
      *(bf16x8*)(p.Xf + ((size_t)(b * 20 + kt) * 64 + l) * 8) = *(const bf16x8*)(sm + (l & 15) * 648 + kt * 32 + (l >> 4) * 8);
    }
    return;
  }
  b -= N_LN;
  if (b < N_KV) {
    const int s0 = (b / 10) * 64, d0 = (b % 10) * 64;
    u16* st = sm;
    const int c8 = t & 7, rr = t >> 3, d = d0 + c8 * 8;
    bf16x8 kk[2][4], vv[2];
#pragma unroll
    for (int h = 0; h < 2; ++h) {
      int s = s0 + rr + 32 * h;
#pragma unroll
      for (int tt = 0; tt < 4; ++tt) {
        int ss = s - 3 + tt;
        kk[h][tt] = bf16x8{0, 0, 0, 0, 0, 0, 0, 0};
        if (ss >= 0) kk[h][tt] = *(const bf16x8*)(p.k + (size_t)ss * D + d);
      }
      vv[h] = *(const bf16x8*)(p.v + (size_t)s * D + d);
    }
#pragma unroll
    for (int j = 0; j < 8; ++j) {
      s16x4 wv = *(const s16x4*)(p.sk + (d + j) * 4);
#pragma unroll
      for (int h = 0; h < 2; ++h) {
        float ac = 0.f;
#pragma unroll
        for (int tt = 0; tt < 4; ++tt) ac = rbf(ac + rbf(bf2f((u16)kk[h][tt][j]) * bf2f((u16)wv[3 - tt])));
        st[(c8 * 8 + j) * 72 + rr + 32 * h] = f2bf(ac * bf2f((u16)vv[h][j]));
      }
    }
    __syncthreads();
#pragma unroll
    for (int i = 0; i < 2; ++i) {
      int id = t + 256 * i, dd = id >> 3, c = id & 7;
      *(bf16x8*)(p.kvT + (size_t)(d0 + dd) * S + s0 + c * 8) = *(const bf16x8*)(st + dd * 72 + c * 8);
    }
    return;
  }
  b -= N_KV;
  if (b < 60) tile_w<576>(p.wg, F, p.Wgf, 20, b / 3, (b % 3) * 576, sm);
  else if (b < 120) tile_w<576>(p.wu, F, p.Wuf, 20, (b - 60) / 3, ((b - 60) % 3) * 576, sm);
  else tile_w<640>(p.wd, D, p.Wdf, 54, b - 120, 0, sm);
}

constexpr int STG = 20480;
constexpr int LDS1 = 4 * STG;
constexpr int ZOFF = 2080, ZLEN = 3200, ZB = (ZLEN / 32) * 40 * 2;
constexpr int CW = 528, CS = 1344, CCH = 8 * CS;
__device__ __forceinline__ int zaddr(int i) { return ((i >> 5) * 40 + (i & 31)) * 2; }

__device__ __forceinline__ void glds16(const void* g, void* l) {
  __builtin_amdgcn_global_load_lds((const __attribute__((address_space(1))) unsigned*)g,
                                   (__attribute__((address_space(3))) unsigned*)l, 16, 0, 0);
}

__device__ __forceinline__ void gemm1(const Params& p, int bid, char* sm) {
  const int t = threadIdx.x, lane = t & 63, w = t >> 6;
  const int xcd = bid & 7, loc = bid >> 3;
  const int mt = xcd * 2 + loc / 18, ft = loc % 18;
  const u16* src[5];
#pragma unroll
  for (int i = 0; i < 5; ++i) {
    int fi = w * 5 + i;
    const u16* base = fi < 8 ? p.Xf + (size_t)(mt * 8 + fi) * 20 * 512
                    : fi < 14 ? p.Wgf + (size_t)(ft * 6 + fi - 8) * 20 * 512
                              : p.Wuf + (size_t)(ft * 6 + fi - 14) * 20 * 512;
    src[i] = base + lane * 8;
  }
  auto stage = [&](int kt) {
#pragma unroll
    for (int i = 0; i < 5; ++i) glds16(src[i] + kt * 512, sm + (kt & 3) * STG + (w * 5 + i) * 1024 + lane * 16);
  };
  const int wm = w >> 1, wn = w & 1;
  f32x4 acc[4][6];
#pragma unroll
  for (int i = 0; i < 4; ++i)
#pragma unroll
    for (int j = 0; j < 6; ++j) acc[i][j] = f32x4{0, 0, 0, 0};
  stage(0); stage(1); stage(2);
#pragma unroll 4
  for (int kt = 0; kt < 20; ++kt) {
    if (kt < 18) asm volatile("s_waitcnt vmcnt(10)" ::: "memory");
    else if (kt == 18) asm volatile("s_waitcnt vmcnt(5)" ::: "memory");
    else asm volatile("s_waitcnt vmcnt(0)" ::: "memory");
    __builtin_amdgcn_s_barrier();
    if (kt + 3 < 20) stage(kt + 3);
    const char* bs = sm + (kt & 3) * STG + lane * 16;
    bf16x8 af[4], bfr[6];
#pragma unroll
    for (int i = 0; i < 4; ++i) af[i] = *(const bf16x8*)(bs + (wm * 4 + i) * 1024);
#pragma unroll
    for (int j = 0; j < 6; ++j) bfr[j] = *(const bf16x8*)(bs + (8 + (j / 3) * 6 + wn * 3 + (j % 3)) * 1024);
#pragma unroll
    for (int i = 0; i < 4; ++i)
#pragma unroll
      for (int j = 0; j < 6; ++j)
        acc[i][j] = __builtin_amdgcn_mfma_f32_16x16x32_bf16(af[i], bfr[j], acc[i][j], 0, 0, 0);
  }
  __syncthreads();
  char* sH = sm;
  const int g = lane >> 4;
#pragma unroll
  for (int i = 0; i < 4; ++i)
#pragma unroll
    for (int j = 0; j < 3; ++j)
#pragma unroll
      for (int e = 0; e < 4; ++e) {
        float gg = rbf(acc[i][j][e]), uu = rbf(acc[i][j + 3][e]);
        float sg = rbf(1.f / (1.f + __expf(-gg)));
        int r = wm * 64 + i * 16 + g * 4 + e, c = wn * 48 + j * 16 + (lane & 15);
        *(u16*)(sH + r * 208 + c * 2) = f2bf(sg * uu);
      }
  __syncthreads();
#pragma unroll
  for (int i = 0; i < 6; ++i) {
    int id = t + 256 * i, blk = id >> 6, l = id & 63;
    int m16 = blk / 3, k32 = blk % 3;
    bf16x8 v = *(const bf16x8*)(sH + (m16 * 16 + (l & 15)) * 208 + (k32 * 32 + (l >> 4) * 8) * 2);
    *(bf16x8*)(p.Hf + ((size_t)((mt * 8 + m16) * 54 + ft * 3 + k32) * 64 + l) * 8) = v;
  }
}

__device__ __forceinline__ void convk(const Params& p, int bid, char* sm) {
  const int t = threadIdx.x, lane = t & 63, w = t >> 6;
  const int d0 = bid * 4, d = d0 + w;
  char* z = sm + w * ZB; char* cp = sm + 4 * ZB + w * CCH;
  const u16* hrow = p.hk + (size_t)d * S;
  bf16x8 kv4[4], hr[4][2][2];
  {
    const u16* src = p.kvT + (size_t)d * S;
#pragma unroll
    for (int i = 0; i < 4; ++i) kv4[i] = *(const bf16x8*)(src + (lane + 64 * i) * 8);
#pragma unroll
    for (int win = 0; win < 4; ++win)
#pragma unroll
      for (int rd = 0; rd < 2; ++rd)
#pragma unroll
        for (int hh = 0; hh < 2; ++hh) {
          int idx = win * CW - 32 + (lane + 64 * rd) * 8 + hh * 8;
          hr[win][rd][hh] = bf16x8{0, 0, 0, 0, 0, 0, 0, 0};
          if (idx >= 0 && idx < S && (rd == 0 || lane < 6)) hr[win][rd][hh] = *(const bf16x8*)(hrow + idx);
        }
  }
  for (int i = lane; i < ZB / 16; i += 64) *(uint4*)(z + i * 16) = make_uint4(0, 0, 0, 0);
#pragma unroll
  for (int i = 0; i < 4; ++i)
#pragma unroll
    for (int j = 0; j < 8; ++j) *(u16*)(z + zaddr(ZOFF - ((lane + 64 * i) * 8 + j))) = (u16)kv4[i][j];
  f32x16 a0, a1;
#pragma unroll
  for (int i = 0; i < 16; ++i) { a0[i] = 0; a1[i] = 0; }
  const int b = lane & 31, g = lane >> 5;
#pragma unroll
  for (int win = 0; win < 4; ++win) {
    const int base = win * CW - 32;
#pragma unroll
    for (int rd = 0; rd < 2; ++rd) {
      int gi = lane + 64 * rd;
      if (gi < 70) {
#pragma unroll
        for (int c = 0; c < 8; ++c) {
          bf16x8 o;
#pragma unroll
          for (int j = 0; j < 8; ++j) o[j] = (c + j < 8) ? hr[win][rd][0][(c + j) & 7] : hr[win][rd][1][(c + j) & 7];
          *(bf16x8*)(cp + c * CS + gi * 16) = o;
        }
      }
    }
    __builtin_amdgcn_wave_barrier();
    const char* ap = cp + (b & 7) * CS + ((b & ~7) + 8 * g) * 2;
#pragma unroll 11
    for (int st = 0; st < 33; ++st) {
      int k0 = base + st * 16;
      bf16x8 af = *(const bf16x8*)(ap + st * 32);
      bf16x8 b1 = *(const bf16x8*)(z + zaddr(ZOFF - 32 * (b + 32) + k0 + 8 * g));
      a1 = __builtin_amdgcn_mfma_f32_32x32x16_bf16(af, b1, a1, 0, 0, 0);
      if (win < 2) {
        bf16x8 b0 = *(const bf16x8*)(z + zaddr(ZOFF - 32 * b + k0 + 8 * g));
        a0 = __builtin_amdgcn_mfma_f32_32x32x16_bf16(af, b0, a0, 0, 0, 0);
      }
    }
    __builtin_amdgcn_wave_barrier();
  }
  __syncthreads();
  u16* so = (u16*)(sm + 4 * ZB);
  float Dd = bf2f(p.Dv[d]);
#pragma unroll
  for (int tl = 0; tl < 2; ++tl)
#pragma unroll
    for (int r = 0; r < 16; ++r) {
      int s = 32 * (b + 32 * tl) + (r & 3) + 8 * (r >> 2) + 4 * g;
      float y = rbf(tl ? a1[r] : a0[r]);
      float kv = bf2f(*(const u16*)(z + zaddr(ZOFF - s)));
      so[s * 4 + w] = f2bf(y + rbf(kv * Dd));
    }
  __syncthreads();
#pragma unroll
  for (int i = 0; i < 8; ++i) { int s = t + 256 * i; *(uint2*)(p.y2 + (size_t)s * D + d0) = *(const uint2*)(so + s * 4); }
}

__global__ __launch_bounds__(256, 2) void k1(Params p) {
  __shared__ __attribute__((aligned(16))) char sm[LDS1];
  int b = blockIdx.x;
  if (b < NCV) convk(p, b, sm);
  else gemm1(p, b - NCV, sm);
}

__global__ __launch_bounds__(256, 1) void k2(Params p) {
  __shared__ __attribute__((aligned(16))) float red[4][64][81];
  const int t = threadIdx.x, lane = t & 63, w = t >> 6;
  const int mt = (blockIdx.x & 7) * 4 + ((blockIdx.x >> 3) & 3), nt = blockIdx.x >> 5;
  f32x4 acc[4][5];
#pragma unroll
  for (int i = 0; i < 4; ++i)
#pragma unroll
    for (int j = 0; j < 5; ++j) acc[i][j] = f32x4{0, 0, 0, 0};
  bf16x8 a[4][4], bb[4][5];
  auto ld = [&](int sl, int kt) {
    if (kt > 53) kt = 53;
#pragma unroll
    for (int i = 0; i < 4; ++i) a[sl][i] = *(const bf16x8*)(p.Hf + ((size_t)((mt * 4 + i) * 54 + kt) * 64 + lane) * 8);
#pragma unroll
    for (int j = 0; j < 5; ++j) bb[sl][j] = *(const bf16x8*)(p.Wdf + ((size_t)((nt * 5 + j) * 54 + kt) * 64 + lane) * 8);
  };
  ld(0, w); ld(1, w + 4); ld(2, w + 8);
  s16x4 xx[5], yy[5]; float2 sts[5];
#pragma unroll
  for (int i = 0; i < 5; ++i) {
    int id = t + 256 * i, r = id / 20, c = (id % 20) * 4;
    size_t o = (size_t)(mt * 64 + r) * D + nt * 80 + c;
    xx[i] = *(const s16x4*)(p.x + o); yy[i] = *(const s16x4*)(p.y2 + o); sts[i] = p.stats[mt * 64 + r];
  }
  __builtin_amdgcn_sched_barrier(0);
#pragma unroll
  for (int it = 0; it < 14; ++it) {
    if (it + 3 < 14) ld((it + 3) % 4, w + 4 * (it + 3));
    __builtin_amdgcn_sched_barrier(0);
    if (w + 4 * it < 54) {
#pragma unroll
      for (int i = 0; i < 4; ++i)
#pragma unroll
        for (int j = 0; j < 5; ++j) acc[i][j] = __builtin_amdgcn_mfma_f32_16x16x32_bf16(a[it % 4][i], bb[it % 4][j], acc[i][j], 0, 0, 0);
    }
  }
#pragma unroll
  for (int i = 0; i < 4; ++i)
#pragma unroll
    for (int j = 0; j < 5; ++j)
#pragma unroll
      for (int e = 0; e < 4; ++e) red[w][i * 16 + (lane >> 4) * 4 + e][j * 16 + (lane & 15)] = acc[i][j][e];
  __syncthreads();
#pragma unroll
  for (int i = 0; i < 5; ++i) {
    int id = t + 256 * i, r = id / 20, c = (id % 20) * 4;
    size_t o = (size_t)(mt * 64 + r) * D + nt * 80 + c;
    s16x4 ov;
#pragma unroll
    for (int e = 0; e < 4; ++e) {
      float glu = rbf(red[0][r][c + e] + red[1][r][c + e] + red[2][r][c + e] + red[3][r][c + e]);
      float xv = bf2f((u16)xx[i][e]);
      float xn = rbf(rbf(xv - sts[i].x) / sts[i].y);
      float av = rbf(xn * bf2f((u16)yy[i][e]));
      ov[e] = (short)f2bf(rbf(xv + av) + glu);
    }
    *(s16x4*)(p.out + o) = ov;
  }
}

static void fill_params(Params& p, void* const* d_in, void* d_out, void* d_ws) {
  p.x = (const u16*)d_in[0]; p.k = (const u16*)d_in[1]; p.v = (const u16*)d_in[2];
  p.sk = (const u16*)d_in[3]; p.hk = (const u16*)d_in[4]; p.Dv = (const u16*)d_in[5];
  p.wg = (const u16*)d_in[7]; p.wu = (const u16*)d_in[8]; p.wd = (const u16*)d_in[9];
  p.out = (u16*)d_out;
  char* ws = (char*)d_ws; const size_t MB = 1024 * 1024;
  p.Hf = (u16*)ws; p.Wdf = (u16*)(ws + 8 * MB); p.y2 = (u16*)(ws + 12 * MB);
  p.stats = (float2*)(ws + 16 * MB); p.Xf = (u16*)(ws + 20 * MB); p.kvT = (u16*)(ws + 24 * MB);
  p.Wgf = (u16*)(ws + 28 * MB); p.Wuf = (u16*)(ws + 32 * MB);
}

extern "C" void kernel_launch(void* const* d_in, const int* in_sizes, int n_in,
                              void* d_out, int out_size, void* d_ws, size_t ws_size,
                              hipStream_t stream) {
  Params p;
  fill_params(p, d_in, d_out, d_ws);
  k0<<<N_LN + N_KV + N_WT, 256, 0, stream>>>(p);
  k1<<<NCV + NG1, 256, 0, stream>>>(p);
  k2<<<256, 256, 0, stream>>>(p);
}
```

```cpp
#include <hip/hip_runtime.h>
#include <hip/hip_bf16.h>
#include <stdint.h>

typedef unsigned short u16;
using bf16x8 = __attribute__((ext_vector_type(8))) short;
using s16x4  = __attribute__((ext_vector_type(4))) short;
using f32x4  = __attribute__((ext_vector_type(4))) float;
using f32x16 = __attribute__((ext_vector_type(16))) float;

constexpr int S = 2048, D = 640, F = 1728;
constexpr int NG1 = 256, NCV = 160;
constexpr int N_LN = 128, N_KV = 320, N_WT = 174;

__device__ __forceinline__ float bf2f(u16 v) { return __uint_as_float((unsigned)v << 16); }
__device__ __forceinline__ u16 f2bf(float f) {
  __hip_bfloat16 b = __float2bfloat16(f);
  return *reinterpret_cast<u16*>(&b);
}
__device__ __forceinline__ float rbf(float f) { return bf2f(f2bf(f)); }

struct Params {
  const u16 *x, *k, *v, *sk, *hk, *Dv, *wg, *wu, *wd;
  u16 *out, *Hf, *Wdf, *y2, *Xf, *kvT, *Wgf, *Wuf;
  float2* stats;
};

template <int NC>
__device__ __forceinline__ void tile_w(const u16* src, int N, u16* dst, int KT, int kt, int n0, u16* s) {
  const int t = threadIdx.x;
  constexpr int RS = NC + 8, CPR = NC / 8, TOT = 32 * CPR;
  bf16x8 v[(TOT + 255) / 256];
#pragma unroll
  for (int i = 0; i < (TOT + 255) / 256; ++i) {
    int id = t + 256 * i;
    if (id < TOT) v[i] = *(const bf16x8*)(src + (size_t)(kt * 32 + id / CPR) * N + n0 + (id % CPR) * 8);
  }
#pragma unroll
  for (int i = 0; i < (TOT + 255) / 256; ++i) {
    int id = t + 256 * i;
    if (id < TOT) *(bf16x8*)(s + (id / CPR) * RS + (id % CPR) * 8) = v[i];
  }
  __syncthreads();
#pragma unroll
  for (int i = 0; i < (TOT + 255) / 256; ++i) {
    int id = t + 256 * i;
    if (id < TOT) {
      int l = id & 63, nt = id >> 6;
      bf16x8 o;
#pragma unroll
      for (int j = 0; j < 8; ++j) o[j] = (short)s[((l >> 4) * 8 + j) * RS + nt * 16 + (l & 15)];
      *(bf16x8*)(dst + ((size_t)((n0 / 16 + nt) * KT + kt) * 64 + l) * 8) = o;
    }
  }
}

__global__ __launch_bounds__(256) void k0(Params p) {
  __shared__ __attribute__((aligned(16))) u16 sm[32 * 648];
  int b = blockIdx.x;
  const int t = threadIdx.x, lane = t & 63, w = t >> 6;
  if (b < N_LN) {
    unsigned raw[4][5];
#pragma unroll
    for (int i = 0; i < 4; ++i)
#pragma unroll
      for (int c = 0; c < 5; ++c) raw[i][c] = *(const unsigned*)(p.x + (size_t)(b * 16 + w * 4 + i) * D + c * 128 + lane * 2);
#pragma unroll
    for (int i = 0; i < 4; ++i) {
      int rl = w * 4 + i;
      float v[10], s = 0.f;
#pragma unroll
      for (int c = 0; c < 5; ++c) { v[2 * c] = bf2f((u16)raw[i][c]); v[2 * c + 1] = bf2f((u16)(raw[i][c] >> 16)); s += v[2 * c] + v[2 * c + 1]; }
      for (int o = 1; o < 64; o <<= 1) s += __shfl_xor(s, o);
      float mean = s * (1.f / D), q = 0.f;
#pragma unroll
      for (int j = 0; j < 10; ++j) { float d = v[j] - mean; q += d * d; }
      for (int o = 1; o < 64; o <<= 1) q += __shfl_xor(q, o);
      float den = rbf(sqrtf(rbf(rbf(q * (1.f / D)) + 1e-5f))), mb = rbf(mean);
      if (lane == 0) p.stats[b * 16 + rl] = make_float2(mb, den);
#pragma unroll
      for (int c = 0; c < 5; ++c)
        *(unsigned*)(sm + rl * 648 + c * 128 + lane * 2) =
            (unsigned)f2bf(rbf(v[2 * c] - mb) / den) | ((unsigned)f2bf(rbf(v[2 * c + 1] - mb) / den) << 16);
    }
    __syncthreads();
#pragma unroll
    for (int i = 0; i < 5; ++i) {
      int id = t + 256 * i, l = id & 63, kt = id >> 6;
      *(bf16x8*)(p.Xf + ((size_t)(b * 20 + kt) * 64 + l) * 8) = *(const bf16x8*)(sm + (l & 15) * 648 + kt * 32 + (l >> 4) * 8);
    }
    return;
  }
  b -= N_LN;
  if (b < N_KV) {
    const int s0 = (b / 10) * 64, d0 = (b % 10) * 64;
    u16* st = sm;
    const int c8 = t & 7, rr = t >> 3, d = d0 + c8 * 8;
    bf16x8 kk[2][4], vv[2];
#pragma unroll
    for (int h = 0; h < 2; ++h) {
      int s = s0 + rr + 32 * h;
#pragma unroll
      for (int tt = 0; tt < 4; ++tt) {
        int ss = s - 3 + tt;
        kk[h][tt] = bf16x8{0, 0, 0, 0, 0, 0, 0, 0};
        if (ss >= 0) kk[h][tt] = *(const bf16x8*)(p.k + (size_t)ss * D + d);
      }
      vv[h] = *(const bf16x8*)(p.v + (size_t)s * D + d);
    }
#pragma unroll
    for (int j = 0; j < 8; ++j) {
      s16x4 wv = *(const s16x4*)(p.sk + (d + j) * 4);
#pragma unroll
      for (int h = 0; h < 2; ++h) {
        float ac = 0.f;
#pragma unroll
        for (int tt = 0; tt < 4; ++tt) ac = rbf(ac + rbf(bf2f((u16)kk[h][tt][j]) * bf2f((u16)wv[3 - tt])));
        st[(c8 * 8 + j) * 72 + rr + 32 * h] = f2bf(ac * bf2f((u16)vv[h][j]));
      }
    }
    __syncthreads();
#pragma unroll
    for (int i = 0; i < 2; ++i) {
      int id = t + 256 * i, dd = id >> 3, c = id & 7;
      *(bf16x8*)(p.kvT + (size_t)(d0 + dd) * S + s0 + c * 8) = *(const bf16x8*)(st + dd * 72 + c * 8);
    }
    return;
  }
  b -= N_KV;
  if (b < 60) tile_w<576>(p.wg, F, p.Wgf, 20, b / 3, (b % 3) * 576, sm);
  else if (b < 120) tile_w<576>(p.wu, F, p.Wuf, 20, (b - 60) / 3, ((b - 60) % 3) * 576, sm);
  else tile_w<640>(p.wd, D, p.Wdf, 54, b - 120, 0, sm);
}

constexpr int STG = 22528;
constexpr int LDS1 = 75008;
constexpr int ZOFF = 2080, ZLEN = 3200, ZB = (ZLEN / 32) * 40 * 2;
constexpr int CW = 528, CS = 1344, CCH = 8 * CS;
__device__ __forceinline__ int zaddr(int i) { return ((i >> 5) * 40 + (i & 31)) * 2; }

__device__ __forceinline__ void glds16(const void* g, void* l) {
  __builtin_amdgcn_global_load_lds((const __attribute__((address_space(1))) unsigned*)g,
                                   (__attribute__((address_space(3))) unsigned*)l, 16, 0, 0);
}

__device__ __forceinline__ void gemm1(const Params& p, int bid, char* sm) {
  const int t = threadIdx.x, lane = t & 63, w = t >> 6;
  const int xcd = bid & 7, loc = bid >> 3;
  const int mt = xcd * 2 + (loc >> 4), ft = loc & 15;
  const int n0 = ft < 12 ? ft * 7 : 84 + (ft - 12) * 6, nb = ft < 12 ? 7 : 6;
  const u16* src[6]; int dst[6];
#pragma unroll
  for (int i = 0; i < 6; ++i) {
    int fi = w + 4 * i; if (fi > 21) fi = 21;
    int nn = fi < 15 ? fi - 8 : fi - 15; nn = n0 + nn; if (nn > 107) nn = 107;
    const u16* base = fi < 8 ? p.Xf + (size_t)(mt * 8 + fi) * 20 * 512
                    : fi < 15 ? p.Wgf + (size_t)nn * 20 * 512 : p.Wuf + (size_t)nn * 20 * 512;
    src[i] = base + lane * 8; dst[i] = fi * 1024;
  }
  auto stage = [&](int kt) {
#pragma unroll
    for (int i = 0; i < 6; ++i) glds16(src[i] + kt * 512, sm + (kt % 3) * STG + dst[i] + lane * 16);
  };
  f32x4 acc[2][14];
#pragma unroll
  for (int i = 0; i < 2; ++i)
#pragma unroll
    for (int j = 0; j < 14; ++j) acc[i][j] = f32x4{0, 0, 0, 0};
  stage(0); stage(1);
#pragma unroll
  for (int kt = 0; kt < 20; ++kt) {
    if (kt < 19) asm volatile("s_waitcnt vmcnt(6)" ::: "memory");
    else asm volatile("s_waitcnt vmcnt(0)" ::: "memory");
    asm volatile("s_barrier" ::: "memory"); __builtin_amdgcn_sched_barrier(0);
    if (kt + 2 < 20) stage(kt + 2);
    const char* bs = sm + (kt % 3) * STG + lane * 16;
    bf16x8 af[2], bfr[14];
#pragma unroll
    for (int i = 0; i < 2; ++i) af[i] = *(const bf16x8*)(bs + (w * 2 + i) * 1024);
#pragma unroll
    for (int j = 0; j < 14; ++j) bfr[j] = *(const bf16x8*)(bs + (8 + j) * 1024);
#pragma unroll
    for (int j = 0; j < 14; ++j)
#pragma unroll
      for (int i = 0; i < 2; ++i)
        acc[i][j] = __builtin_amdgcn_mfma_f32_16x16x32_bf16(af[i], bfr[j], acc[i][j], 0, 0, 0);
  }
  __syncthreads();
  char* sH = sm;
  const int g = lane >> 4;
#pragma unroll
  for (int i = 0; i < 2; ++i)
#pragma unroll
    for (int j = 0; j < 7; ++j)
#pragma unroll
      for (int e = 0; e < 4; ++e) {
        float gg = rbf(acc[i][j][e]), uu = rbf(acc[i][j + 7][e]);
        float sg = rbf(1.f / (1.f + __expf(-gg)));
        int r = w * 32 + i * 16 + g * 4 + e, c = j * 16 + (lane & 15);
        *(u16*)(sH + r * 240 + c * 2) = f2bf(sg * uu);
      }
  __syncthreads();
#pragma unroll
  for (int i = 0; i < 7; ++i) {
    int id = t + 256 * i, r = id & 127, c8 = id >> 7;
    if (c8 < nb * 2) {
      int f = n0 * 16 + c8 * 8, row = mt * 128 + r;
      bf16x8 v = *(const bf16x8*)(sH + r * 240 + c8 * 16);
      *(bf16x8*)(p.Hf + ((size_t)((row >> 4) * 54 + (f >> 5)) * 64 + (row & 15) + 16 * ((f & 31) >> 3)) * 8) = v;
    }
  }
}

__device__ __forceinline__ void convk(const Params& p, int bid, char* sm) {
  const int t = threadIdx.x, lane = t & 63, w = t >> 6;
  const int d0 = bid * 4, d = d0 + w;
  char* z = sm + w * ZB; char* cp = sm + 4 * ZB + w * CCH;
  const u16* hrow = p.hk + (size_t)d * S;
  bf16x8 kv4[4], hr[4][2][2];
  {
    const u16* src = p.kvT + (size_t)d * S;
#pragma unroll
    for (int i = 0; i < 4; ++i) kv4[i] = *(const bf16x8*)(src + (lane + 64 * i) * 8);
#pragma unroll
    for (int win = 0; win < 4; ++win)
#pragma unroll
      for (int rd = 0; rd < 2; ++rd)
#pragma unroll
        for (int hh = 0; hh < 2; ++hh) {
          int idx = win * CW - 32 + (lane + 64 * rd) * 8 + hh * 8;
          hr[win][rd][hh] = bf16x8{0, 0, 0, 0, 0, 0, 0, 0};
          if (idx >= 0 && idx < S && (rd == 0 || lane < 6)) hr[win][rd][hh] = *(const bf16x8*)(hrow + idx);
        }
  }
  for (int i = lane; i < ZB / 16; i += 64) *(uint4*)(z + i * 16) = make_uint4(0, 0, 0, 0);
#pragma unroll
  for (int i = 0; i < 4; ++i)
#pragma unroll
    for (int j = 0; j < 8; ++j) *(u16*)(z + zaddr(ZOFF - ((lane + 64 * i) * 8 + j))) = (u16)kv4[i][j];
  f32x16 a0, a1;
#pragma unroll
  for (int i = 0; i < 16; ++i) { a0[i] = 0; a1[i] = 0; }
  const int b = lane & 31, g = lane >> 5;
#pragma unroll
  for (int win = 0; win < 4; ++win) {
    const int base = win * CW - 32;
#pragma unroll
    for (int rd = 0; rd < 2; ++rd) {
      int gi = lane + 64 * rd;
      if (gi < 70) {
#pragma unroll
        for (int c = 0; c < 8; ++c) {
          bf16x8 o;
#pragma unroll
          for (int j = 0; j < 8; ++j) o[j] = (c + j < 8) ? hr[win][rd][0][(c + j) & 7] : hr[win][rd][1][(c + j) & 7];
          *(bf16x8*)(cp + c * CS + gi * 16) = o;
        }
      }
    }
    __builtin_amdgcn_wave_barrier();
    const char* ap = cp + (b & 7) * CS + ((b & ~7) + 8 * g) * 2;
    bf16x8 fa[2], f1[2], f0[2];
    auto crd = [&](int sl, int st) {
      int k0 = base + st * 16;
      fa[sl] = *(const bf16x8*)(ap + st * 32);
      f1[sl] = *(const bf16x8*)(z + zaddr(ZOFF - 32 * (b + 32) + k0 + 8 * g));
      if (win < 2) f0[sl] = *(const bf16x8*)(z + zaddr(ZOFF - 32 * b + k0 + 8 * g));
    };
    crd(0, 0);
#pragma unroll
    for (int st = 0; st < 33; ++st) {
      if (st + 1 < 33) crd((st + 1) & 1, st + 1);
      __builtin_amdgcn_sched_barrier(0);
      a1 = __builtin_amdgcn_mfma_f32_32x32x16_bf16(fa[st & 1], f1[st & 1], a1, 0, 0, 0);
      if (win < 2) a0 = __builtin_amdgcn_mfma_f32_32x32x16_bf16(fa[st & 1], f0[st & 1], a0, 0, 0, 0);
      __builtin_amdgcn_sched_barrier(0);
    }
    __builtin_amdgcn_wave_barrier();
  }
  __syncthreads();
  u16* so = (u16*)(sm + 4 * ZB);
  float Dd = bf2f(p.Dv[d]);
#pragma unroll
  for (int tl = 0; tl < 2; ++tl)
#pragma unroll
    for (int r = 0; r < 16; ++r) {
      int s = 32 * (b + 32 * tl) + (r & 3) + 8 * (r >> 2) + 4 * g;
      float y = rbf(tl ? a1[r] : a0[r]);
      float kv = bf2f(*(const u16*)(z + zaddr(ZOFF - s)));
      so[(s + (s >> 5)) * 4 + w] = f2bf(y + rbf(kv * Dd));
    }
  __syncthreads();
#pragma unroll
  for (int i = 0; i < 8; ++i) { int s = t + 256 * i; *(uint2*)(p.y2 + (size_t)s * D + d0) = *(const uint2*)(so + (s + (s >> 5)) * 4); }
}

__global__ __launch_bounds__(256, 2) void k1(Params p) {
  __shared__ __attribute__((aligned(16))) char sm[LDS1];
  int b = blockIdx.x;
  if (b < NCV) convk(p, b, sm);
  else gemm1(p, b - NCV, sm);
}

__global__ __launch_bounds__(256, 1) void k2(Params p) {
  __shared__ __attribute__((aligned(16))) float red[4][64][81];
  const int t = threadIdx.x, lane = t & 63, w = t >> 6;
  const int mt = (blockIdx.x & 7) * 4 + ((blockIdx.x >> 3) & 3), nt = blockIdx.x >> 5;
  f32x4 acc[4][5];
#pragma unroll
  for (int i = 0; i < 4; ++i)
#pragma unroll
    for (int j = 0; j < 5; ++j) acc[i][j] = f32x4{0, 0, 0, 0};
  bf16x8 a[4][4], bb[4][5];
  auto ld = [&](int sl, int kt) {
    if (kt > 53) kt = 53;
#pragma unroll
    for (int i = 0; i < 4; ++i) a[sl][i] = *(const bf16x8*)(p.Hf + ((size_t)((mt * 4 + i) * 54 + kt) * 64 + lane) * 8);
#pragma unroll
    for (int j = 0; j < 5; ++j) bb[sl][j] = *(const bf16x8*)(p.Wdf + ((size_t)((nt * 5 + j) * 54 + kt) * 64 + lane) * 8);
  };
  ld(0, w); ld(1, w + 4); ld(2, w + 8);
  s16x4 xx[5], yy[5]; float2 sts[5];
#pragma unroll
  for (int i = 0; i < 5; ++i) {
    int id = t + 256 * i, r = id / 20, c = (id % 20) * 4;
    size_t o = (size_t)(mt * 64 + r) * D + nt * 80 + c;
    xx[i] = *(const s16x4*)(p.x + o); yy[i] = *(const s16x4*)(p.y2 + o); sts[i] = p.stats[mt * 64 + r];
  }
  __builtin_amdgcn_sched_barrier(0);
#pragma unroll
  for (int it = 0; it < 14; ++it) {
    if (it + 3 < 14) ld((it + 3) % 4, w + 4 * (it + 3));
    __builtin_amdgcn_sched_barrier(0);
    if (w + 4 * it < 54) {
#pragma unroll
      for (int i = 0; i < 4; ++i)
#pragma unroll
        for (int j = 0; j < 5; ++j) acc[i][j] = __builtin_amdgcn_mfma_f32_16x16x32_bf16(a[it % 4][i], bb[it % 4][j], acc[i][j], 0, 0, 0);
    }
  }
#pragma unroll
  for (int i = 0; i < 4; ++i)
#pragma unroll
    for (int j = 0; j < 5; ++j)
#pragma unroll
      for (int e = 0; e < 4; ++e) red[w][i * 16 + (lane >> 4) * 4 + e][j * 16 + (lane & 15)] = acc[i][j][e];
  __syncthreads();
#pragma unroll
  for (int i = 0; i < 5; ++i) {
    int id = t + 256 * i, r = id / 20, c = (id % 20) * 4;
    size_t o = (size_t)(mt * 64 + r) * D + nt * 80 + c;
    s16x4 ov;
#pragma unroll
    for (int e = 0; e < 4; ++e) {
      float glu = rbf(red[0][r][c + e] + red[1][r][c + e] + red[2][r][c + e] + red[3][r][c + e]);
      float xv = bf2f((u16)xx[i][e]);
      float xn = rbf(rbf(xv - sts[i].x) / sts[i].y);
      float av = rbf(xn * bf2f((u16)yy[i][e]));
      ov[e] = (short)f2bf(rbf(xv + av) + glu);
    }
    *(s16x4*)(p.out + o) = ov;
  }
}

static void fill_params(Params& p, void* const* d_in, void* d_out, void* d_ws) {
  p.x = (const u16*)d_in[0]; p.k = (const u16*)d_in[1]; p.v = (const u16*)d_in[2];
  p.sk = (const u16*)d_in[3]; p.hk = (const u16*)d_in[4]; p.Dv = (const u16*)d_in[5];
  p.wg = (const u16*)d_in[7]; p.wu = (const u16*)d_in[8]; p.wd = (const u16*)d_in[9];
  p.out = (u16*)d_out;
  char* ws = (char*)d_ws; const size_t MB = 1024 * 1024;
  p.Hf = (u16*)ws; p.Wdf = (u16*)(ws + 8 * MB); p.y2 = (u16*)(ws + 12 * MB);
  p.stats = (float2*)(ws + 16 * MB); p.Xf = (u16*)(ws + 20 * MB); p.kvT = (u16*)(ws + 24 * MB);
  p.Wgf = (u16*)(ws + 28 * MB); p.Wuf = (u16*)(ws + 32 * MB);
}

extern "C" void kernel_launch(void* const* d_in, const int* in_sizes, int n_in,
                              void* d_out, int out_size, void* d_ws, size_t ws_size,
                              hipStream_t stream) {
  Params p;
  fill_params(p, d_in, d_out, d_ws);
  k0<<<N_LN + N_KV + N_WT, 256, 0, stream>>>(p);
  k1<<<NCV + NG1, 256, 0, stream>>>(p);
  k2<<<256, 256, 0, stream>>>(p);
}
```
